```python
import torch

def _softmax(x, axis=-1):
    return torch.softmax(x, dim=axis)
def _gelu(x):
    return torch.nn.functional.gelu(x, approximate="tanh")
def _silu(x):
    return torch.nn.functional.silu(x)
def _sigmoid(x):
    return torch.sigmoid(x)
def _layernorm(x, axis=-1, eps=1e-5):
    m = x.mean(dim=axis, keepdim=True)
    v = x.var(dim=axis, keepdim=True, unbiased=False)
    return (x - m) / torch.sqrt(v + eps)
def _scatter_add(tgt, idx, src, axis=0):
    return tgt.clone().index_add_(axis, idx, src)
def _segment_sum(src, seg, n):
    out = torch.zeros((n,) + src.shape[1:], dtype=src.dtype)
    return out.index_add_(0, seg, src)
def _pad(x, pw):
    flat = tuple(v for pair in reversed(pw) for v in pair)
    return torch.nn.functional.pad(x, flat)

kernel_name = 'standalone__weight_only_int_quip__prompt'

def setup_inputs():

    _w = (lambda a: (lambda t: t.to(torch.bfloat16) if t.is_floating_point() else t)(torch.as_tensor(a)))
    B, S, D, F = 1, 2048, 512, 1376
    G = 128 if 128 > 0 else D
    D = (D // G) * G
    ng = D // G
    bits = 4 if 4 > 0 else 4
    qmax = 1 << bits
    qw = torch.randint(0, qmax, (D, F)).to(torch.int32)
    U, _ = (lambda *_a, **_k: torch.linalg.qr(*[torch.as_tensor(_x).float() for _x in _a], **_k))((lambda *s: torch.randn(*s, dtype=torch.bfloat16) * 0.05)(D, D))
    V, _ = (lambda *_a, **_k: torch.linalg.qr(*[torch.as_tensor(_x).float() for _x in _a], **_k))((lambda *s: torch.randn(*s, dtype=torch.bfloat16) * 0.05)(F, F))
    return {
        "x": _w((lambda *s: torch.randn(*s, dtype=torch.bfloat16) * 0.05)(B, S, D)),
        "qw": _w(qw),
        "scales": _w(torch.abs((lambda *s: torch.randn(*s, dtype=torch.bfloat16) * 0.05)(ng, F)) * 0.02 + 0.005),
        "zeros": _w(torch.full((ng, F), qmax / 2.0, dtype=float)),
        "U": _w(U.to(float)),
        "V": _w(V.to(float)),
    }

def reference(x, qw, scales, zeros, U, V):
    B, S, D = x.shape
    ng, F = scales.shape
    G = D // ng
    s = (lambda x, n, axis: torch.repeat_interleave(x, n, dim=axis))(scales, G, axis=0)
    z = (lambda x, n, axis: torch.repeat_interleave(x, n, dim=axis))(zeros, G, axis=0)
    wq = ((lambda x, dt: x.to(dt))(qw, x.dtype) - z) * s
    xr = torch.einsum("bsd,de->bse", x, U)
    h  = torch.einsum("bse,ef->bsf", xr, wq)
    y  = torch.einsum("bsf,fg->bsg", h, V)
    return y
```

```cpp
#include <hip/hip_runtime.h>
#include <hip/hip_bf16.h>
typedef __hip_bfloat16 bf16;
constexpr int S_=2048, D_=512, F_=1376, G_=128;

__global__ void kT(const int* qw, const bf16* sc, const bf16* ze, const bf16* V, bf16* T){
  int n = blockIdx.x*64+threadIdx.x; int d = blockIdx.y;
  if(n>=F_) return;
  int g = d/G_;
  float acc=0;
  for(int f=0;f<F_;++f){
    float q = __bfloat162float(__float2bfloat16((float)qw[d*F_+f]));
    float a = __bfloat162float(__float2bfloat16(q-__bfloat162float(ze[g*F_+f])));
    float w = __bfloat162float(__float2bfloat16(a*__bfloat162float(sc[g*F_+f])));
    acc += w*__bfloat162float(V[f*F_+n]);
  }
  T[d*F_+n]=__float2bfloat16(acc);
}
__global__ void kX(const bf16* x, const bf16* U, bf16* xr){
  int e = blockIdx.x*64+threadIdx.x; int s = blockIdx.y;
  float acc=0;
  for(int d=0;d<D_;++d) acc += __bfloat162float(x[s*D_+d])*__bfloat162float(U[d*D_+e]);
  xr[s*D_+e]=__float2bfloat16(acc);
}
__global__ void kY(const bf16* xr, const bf16* T, bf16* y){
  int n = blockIdx.x*64+threadIdx.x; int s = blockIdx.y;
  if(n>=F_) return;
  float acc=0;
  for(int d=0;d<D_;++d) acc += __bfloat162float(xr[s*D_+d])*__bfloat162float(T[d*F_+n]);
  y[s*F_+n]=__float2bfloat16(acc);
}
extern "C" void kernel_launch(void* const* d_in, const int* in_sizes, int n_in,
                              void* d_out, int out_size, void* d_ws, size_t ws_size,
                              hipStream_t stream) {
  const bf16* x=(const bf16*)d_in[0]; const int* qw=(const int*)d_in[1];
  const bf16* sc=(const bf16*)d_in[2]; const bf16* ze=(const bf16*)d_in[3];
  const bf16* U=(const bf16*)d_in[4]; const bf16* V=(const bf16*)d_in[5];
  bf16* T=(bf16*)d_ws; bf16* xr=T+D_*F_;
  kT<<<dim3(22,D_),64,0,stream>>>(qw,sc,ze,V,T);
  kX<<<dim3(8,S_),64,0,stream>>>(x,U,xr);
  kY<<<dim3(22,S_),64,0,stream>>>(xr,T,(bf16*)d_out);
}
```

```cpp
#include <hip/hip_runtime.h>
#include <hip/hip_bf16.h>

typedef __attribute__((ext_vector_type(8))) __bf16 bf16x8;
typedef __attribute__((ext_vector_type(4))) short s16x4;
typedef __attribute__((ext_vector_type(8))) short s16x8;
typedef __attribute__((ext_vector_type(16))) float f32x16;
typedef __attribute__((ext_vector_type(4))) float f32x4;
typedef __attribute__((ext_vector_type(4))) unsigned u32x4;
typedef __attribute__((ext_vector_type(2))) unsigned u32x2;
typedef __attribute__((ext_vector_type(4))) int i32x4;
typedef __attribute__((ext_vector_type(2))) __bf16 bf16x2;
typedef __attribute__((ext_vector_type(2))) float f32x2;
#define LAS __attribute__((address_space(3)))
#define GAS __attribute__((address_space(1)))

constexpr int S_ = 2048, D_ = 512, F_ = 1376;
constexpr int NCB = 43;
constexpr int TF_HALF = NCB * 32 * 1024;
constexpr int XF_BYTES = 64 * 32 * 1024;

#define BAR() asm volatile("s_waitcnt lgkmcnt(0)\n\ts_barrier" ::: "memory")
#define WAIT_V(n) asm volatile("s_waitcnt vmcnt(%0)" ::"n"(n) : "memory")
#define GLDS(src, dst) __builtin_amdgcn_global_load_lds((const GAS unsigned*)(src), (LAS unsigned*)(dst), 16, 0, 0)

__device__ __forceinline__ unsigned pk2(float a, float b) {
  f32x2 v = {a, b};
  return __builtin_bit_cast(unsigned, __builtin_convertvector(v, bf16x2));
}
__device__ __forceinline__ float bflo(unsigned u) { return __builtin_bit_cast(float, u << 16); }
__device__ __forceinline__ float bfhi(unsigned u) { return __builtin_bit_cast(float, u & 0xffff0000u); }
__device__ __forceinline__ s16x4 trd(LAS char* p) {
  return __builtin_amdgcn_ds_read_tr16_b64_v4i16((LAS s16x4*)p);
}
__device__ __forceinline__ bf16x8 cat(s16x4 a, s16x4 b) {
  s16x8 r = __builtin_shufflevector(a, b, 0, 1, 2, 3, 4, 5, 6, 7);
  return __builtin_bit_cast(bf16x8, r);
}
__device__ __forceinline__ u32x4 packh(const f32x16& a, int o) {
  u32x4 r = {pk2(a[o], a[o + 1]), pk2(a[o + 2], a[o + 3]), pk2(a[o + 4], a[o + 5]), pk2(a[o + 6], a[o + 7])};
  return r;
}
#define MFMA(a, b, c) __builtin_amdgcn_mfma_f32_32x32x16_bf16(a, b, c, 0, 0, 0)

constexpr int TD = 4;
constexpr int TNS = TD + 1;
constexpr int T_WQ = TNS * 16384;
constexpr int T_SC = T_WQ + 16384;
constexpr int XD = 3, XNS = XD + 1;
constexpr int K1_LDS = XNS * 32768;
static_assert(T_SC + 704 * 8 <= K1_LDS, "lds");

__global__ __launch_bounds__(512, 2) void k1(const char* __restrict__ x, const char* __restrict__ qw,
                                             const char* __restrict__ sc, const char* __restrict__ ze,
                                             const char* __restrict__ U, const char* __restrict__ V,
                                             char* __restrict__ Tf, char* __restrict__ Xf) {
  extern __shared__ __attribute__((aligned(1024))) char shm_[];
  LAS char* shm = (LAS char*)shm_;
  const int tid = threadIdx.x, lane = tid & 63;
  const int wv = __builtin_amdgcn_readfirstlane(tid >> 6);
  const int xcd = blockIdx.x & 7, idx = blockIdx.x >> 3;
  const int h = lane >> 5, r31 = lane & 31;
  const int q = (lane & 15) >> 2, p = lane & 3, blk = (lane >> 4) & 1;
  auto troff = [&](int c, int t) {
    return 256 * (8 * h + 4 * t + q) + 16 * (((c ^ q) << 2) | ((2 * blk + (p >> 1)) ^ (2 * h + t))) + 8 * (p & 1);
  };
  auto nroff = [&](int r, int ks) { return 128 * r + 16 * ((2 * ks + h) ^ ((r >> 1) & 7)); };

  if (xcd < 6) {
    const int kh = xcd / 3, third = xcd % 3;
    const int mg = idx & 7, ng = third * 4 + (idx >> 3);
    if (ng > 10) return;
    const int n0 = min(ng * 128, F_ - 128);
    const int k0 = kh * 672;
    if (wv >= 4) {
      const int lw = wv - 4;
      const char* vsrc[4];
      const char* qsrc[4];
      int woff[4];
#pragma unroll
      for (int j = 0; j < 4; ++j) {
        int row = 16 * lw + 4 * j + (lane >> 4);
        int ch = (lane & 15) ^ (((row & 3) << 2) | ((row >> 2) & 3));
        vsrc[j] = V + (size_t)(k0 + row) * (F_ * 2) + n0 * 2 + ch * 16;
        qsrc[j] = qw + (size_t)(mg * 64 + row) * (F_ * 4) + k0 * 4 + (lane & 15) * 16;
        woff[j] = T_WQ + 128 * row + 16 * (((lane & 15) >> 1) ^ ((row >> 1) & 7)) + 8 * (lane & 1);
      }
      i32x4 qr[TD][4];
      auto issue = [&](int st) {
#pragma unroll
        for (int j = 0; j < 4; ++j)
          GLDS(vsrc[j] + (size_t)st * 64 * (F_ * 2), shm + (st % TNS) * 16384 + (4 * lw + j) * 1024);
#pragma unroll
        for (int j = 0; j < 4; ++j) qr[st % TD][j] = *(const GAS i32x4*)(qsrc[j] + st * 256);
        __builtin_amdgcn_sched_barrier(0);
      };
      auto dequant = [&](int st) {
        f32x4 s = *(LAS f32x4*)(shm + T_SC + st * 256 + (lane & 15) * 16);
        f32x4 m = *(LAS f32x4*)(shm + T_SC + 2816 + st * 256 + (lane & 15) * 16);
#pragma unroll
        for (int j = 0; j < 4; ++j) {
          i32x4 v = qr[st % TD][j];
          u32x2 o = {pk2(__builtin_fmaf((float)v[0], s[0], m[0]), __builtin_fmaf((float)v[1], s[1], m[1])),
                     pk2(__builtin_fmaf((float)v[2], s[2], m[2]), __builtin_fmaf((float)v[3], s[3], m[3]))};
          *(LAS u32x2*)(shm + woff[j] + (st & 1) * 8192) = o;
        }
      };
#pragma unroll
      for (int st = 0; st < TD; ++st) issue(st);
      BAR();
      dequant(0);
      BAR();
#pragma unroll
      for (int st = 0; st < 11; ++st) {
        if (st + TD < 11) issue(st + TD);
        if (st + 1 < 11) { dequant(st + 1); BAR(); }
      }
    } else {
      const int mh = wv & 1, nh = wv >> 1;
      if (tid < 88) {
        const int g = mg >> 1;
        u32x4 sv = *(const GAS u32x4*)(sc + g * (F_ * 2) + k0 * 2 + tid * 16);
        u32x4 zv = *(const GAS u32x4*)(ze + g * (F_ * 2) + k0 * 2 + tid * 16);
        if (kh == 1 && tid < 4) { sv = u32x4{0, 0, 0, 0}; zv = sv; }
        f32x4 s0 = {bflo(sv[0]), bfhi(sv[0]), bflo(sv[1]), bfhi(sv[1])}, s1 = {bflo(sv[2]), bfhi(sv[2]), bflo(sv[3]), bfhi(sv[3])};
        f32x4 z0 = {bflo(zv[0]), bfhi(zv[0]), bflo(zv[1]), bfhi(zv[1])}, z1 = {bflo(zv[2]), bfhi(zv[2]), bflo(zv[3]), bfhi(zv[3])};
        *(LAS f32x4*)(shm + T_SC + tid * 32) = s0;
        *(LAS f32x4*)(shm + T_SC + tid * 32 + 16) = s1;
        *(LAS f32x4*)(shm + T_SC + 2816 + tid * 32) = -(z0 * s0);
        *(LAS f32x4*)(shm + T_SC + 2816 + tid * 32 + 16) = -(z1 * s1);
      }
      int ao[4], bo[2][2];
#pragma unroll
      for (int ks = 0; ks < 4; ++ks) ao[ks] = T_WQ + nroff(32 * mh + r31, ks);
#pragma unroll
      for (int c = 0; c < 2; ++c)
#pragma unroll
        for (int t = 0; t < 2; ++t) bo[c][t] = troff(2 * nh + c, t);
      f32x16 acc[2] = {};
      BAR();
      BAR();
#pragma unroll
      for (int st = 0; st < 11; ++st) {
        bf16x8 a[4], b[4][2];
#pragma unroll
        for (int ks = 0; ks < 4; ++ks) {
          a[ks] = *(LAS bf16x8*)(shm + ao[ks] + (st & 1) * 8192);
#pragma unroll
          for (int c = 0; c < 2; ++c)
            b[ks][c] = cat(trd(shm + bo[c][0] + (st % TNS) * 16384 + ks * 4096), trd(shm + bo[c][1] + (st % TNS) * 16384 + ks * 4096));
        }
#pragma unroll
        for (int ks = 0; ks < 4; ++ks)
#pragma unroll
          for (int c = 0; c < 2; ++c) acc[c] = MFMA(a[ks], b[ks][c], acc[c]);
        if (st + 1 < 11) BAR();
      }
      const int db = mg * 2 + mh;
#pragma unroll
      for (int c = 0; c < 2; ++c) {
        const int cb = n0 / 32 + 2 * nh + c;
        if (cb >= ng * 4) {
          char* o = Tf + (size_t)kh * TF_HALF + (size_t)cb * 32768 + db * 2048 + lane * 16;
          *(GAS u32x4*)o = packh(acc[c], 0);
          *(GAS u32x4*)(o + 1024) = packh(acc[c], 8);
        }
      }
    }
  } else {
    const int stile = (xcd - 6) * 8 + (idx & 7), et = idx >> 3;
    if (wv >= 4) {
      const int lw = wv - 4;
      const char* usrc[4];
      const char* xsrc[4];
#pragma unroll
      for (int j = 0; j < 4; ++j) {
        int row = 16 * lw + 4 * j + (lane >> 4);
        int ch = (lane & 15) ^ (((row & 3) << 2) | ((row >> 2) & 3));
        usrc[j] = U + row * (D_ * 2) + et * 256 + ch * 16;
        int xr_ = 32 * lw + 8 * j + (lane >> 3);
        xsrc[j] = x + (size_t)(stile * 128 + xr_) * (D_ * 2) + (((lane & 7) ^ ((xr_ >> 1) & 7)) * 16);
      }
      auto issue = [&](int st) {
#pragma unroll
        for (int j = 0; j < 4; ++j) {
          GLDS(usrc[j] + st * 64 * (D_ * 2), shm + (st % XNS) * 32768 + (4 * lw + j) * 1024);
          GLDS(xsrc[j] + st * 128, shm + (st % XNS) * 32768 + 16384 + (4 * lw + j) * 1024);
        }
      };
#pragma unroll
      for (int st = 0; st < XD; ++st) issue(st);
      WAIT_V(8 * (XD - 1));
      BAR();
#pragma unroll
      for (int st = 0; st < 7; ++st) {
        if (st + XD < 8) { issue(st + XD); WAIT_V(8 * (XD - 1)); }
        else if (st + XD == 8) WAIT_V(8 * (XD - 2));
        else WAIT_V(0);
        BAR();
      }
    } else {
      const int eh = wv & 1, sh = wv >> 1;
      int xo[2][4], uo[2][2];
#pragma unroll
      for (int j = 0; j < 2; ++j)
#pragma unroll
        for (int ks = 0; ks < 4; ++ks) xo[j][ks] = 16384 + nroff(64 * sh + 32 * j + r31, ks);
#pragma unroll
      for (int c = 0; c < 2; ++c)
#pragma unroll
        for (int t = 0; t < 2; ++t) uo[c][t] = troff(2 * eh + c, t);
      f32x16 acc[2][2] = {};
      BAR();
#pragma unroll
      for (int st = 0; st < 8; ++st) {
#pragma unroll
        for (int ks = 0; ks < 4; ++ks) {
          bf16x8 a[2], b[2];
#pragma unroll
          for (int c = 0; c < 2; ++c) {
            a[c] = cat(trd(shm + uo[c][0] + (st % XNS) * 32768 + ks * 4096), trd(shm + uo[c][1] + (st % XNS) * 32768 + ks * 4096));
            b[c] = *(LAS bf16x8*)(shm + xo[c][ks] + (st % XNS) * 32768);
          }
#pragma unroll
          for (int c = 0; c < 2; ++c)
#pragma unroll
            for (int j = 0; j < 2; ++j) acc[c][j] = MFMA(a[c], b[j], acc[c][j]);
        }
        if (st + 1 < 8) BAR();
      }
#pragma unroll
      for (int c = 0; c < 2; ++c)
#pragma unroll
        for (int j = 0; j < 2; ++j) {
          const int eb = et * 4 + 2 * eh + c, sb = stile * 4 + 2 * sh + j;
          char* o = Xf + (size_t)sb * 32768 + eb * 2048 + lane * 16;
          *(GAS u32x4*)o = packh(acc[c][j], 0);
          *(GAS u32x4*)(o + 1024) = packh(acc[c][j], 8);
        }
    }
  }
}

constexpr int K2_LDS = 96 * 1024;
__global__ __launch_bounds__(512, 2) void k2(const char* __restrict__ Tf, const char* __restrict__ Xf, char* __restrict__ y) {
  extern __shared__ __attribute__((aligned(1024))) char shm_[];
  LAS char* shm = (LAS char*)shm_;
  const int tid = threadIdx.x, lane = tid & 63;
  const int wv = __builtin_amdgcn_readfirstlane(tid >> 6);
  const int xcd = blockIdx.x & 7, idx = blockIdx.x >> 3;
  const int stile = (xcd & 1) * 8 + (idx & 7), ng = (xcd >> 1) * 4 + (idx >> 3);
  if (ng > 14) return;
  const int cb0 = min(ng * 3, NCB - 3);
  if (wv >= 4) {
    const int lw = wv - 4;
    const char* src = Tf + (size_t)cb0 * 32768 + lw * 1024 + lane * 16;
    u32x4 t0[8][3], t1[8][3];
#pragma unroll
    for (int st = 0; st < 8; ++st) {
#pragma unroll
      for (int c = 0; c < 3; ++c) {
        t0[st][c] = *(const GAS u32x4*)(src + c * 32768 + st * 4096);
        t1[st][c] = *(const GAS u32x4*)(src + TF_HALF + c * 32768 + st * 4096);
      }
      __builtin_amdgcn_sched_barrier(0);
    }
#pragma unroll
    for (int st = 0; st < 8; ++st) {
#pragma unroll
      for (int c = 0; c < 3; ++c) {
        u32x4 o;
#pragma unroll
        for (int i = 0; i < 4; ++i)
          o[i] = pk2(bflo(t0[st][c][i]) + bflo(t1[st][c][i]), bfhi(t0[st][c][i]) + bfhi(t1[st][c][i]));
        *(LAS u32x4*)(shm + ((4 * st + lw) * 3 + c) * 1024 + lane * 16) = o;
      }
      BAR();
    }
  } else {
    const char* src = Xf + (size_t)(stile * 4 + wv) * 32768 + lane * 16;
    bf16x8 xa[8][4];
    constexpr int PD = 3;
#pragma unroll
    for (int st = 0; st < PD; ++st)
#pragma unroll
      for (int ks = 0; ks < 4; ++ks) xa[st][ks] = *(const GAS bf16x8*)(src + (4 * st + ks) * 1024);
    __builtin_amdgcn_sched_barrier(0);
    f32x16 acc[3] = {};
#pragma unroll
    for (int st = 0; st < 8; ++st) {
      BAR();
      if (st + PD < 8) {
#pragma unroll
        for (int ks = 0; ks < 4; ++ks) xa[st + PD][ks] = *(const GAS bf16x8*)(src + (4 * (st + PD) + ks) * 1024);
        __builtin_amdgcn_sched_barrier(0);
      }
#pragma unroll
      for (int ks = 0; ks < 4; ++ks)
#pragma unroll
        for (int c = 0; c < 3; ++c) {
          bf16x8 t = *(LAS bf16x8*)(shm + ((4 * st + ks) * 3 + c) * 1024 + lane * 16);
          acc[c] = MFMA(t, xa[st][ks], acc[c]);
        }
    }
    char* yo = y + (size_t)(stile * 128 + wv * 32 + (lane & 31)) * (F_ * 2) + cb0 * 64 + (lane >> 5) * 16;
#pragma unroll
    for (int c = 0; c < 3; ++c) {
      if (cb0 + c >= ng * 3) {
#pragma unroll
        for (int g = 0; g < 2; ++g) {
          unsigned a0 = pk2(acc[c][8 * g + 0], acc[c][8 * g + 1]), a1 = pk2(acc[c][8 * g + 2], acc[c][8 * g + 3]);
          unsigned b0 = pk2(acc[c][8 * g + 4], acc[c][8 * g + 5]), b1 = pk2(acc[c][8 * g + 6], acc[c][8 * g + 7]);
          auto r0 = __builtin_amdgcn_permlane32_swap(a0, b0, false, false);
          auto r1 = __builtin_amdgcn_permlane32_swap(a1, b1, false, false);
          u32x4 o = {r0[0], r1[0], r0[1], r1[1]};
          *(GAS u32x4*)(yo + c * 64 + g * 32) = o;
        }
      }
    }
  }
}

extern "C" void kernel_launch(void* const* d_in, const int* in_sizes, int n_in, void* d_out, int out_size,
                              void* d_ws, size_t ws_size, hipStream_t stream) {
  char* Tf = (char*)d_ws;
  char* Xf = Tf + 2 * TF_HALF;
  (void)hipFuncSetAttribute((const void*)k1, hipFuncAttributeMaxDynamicSharedMemorySize, K1_LDS);
  (void)hipFuncSetAttribute((const void*)k2, hipFuncAttributeMaxDynamicSharedMemorySize, K2_LDS);
  k1<<<256, 512, K1_LDS, stream>>>((const char*)d_in[0], (const char*)d_in[1], (const char*)d_in[2], (const char*)d_in[3],
                                   (const char*)d_in[4], (const char*)d_in[5], Tf, Xf);
  k2<<<256, 512, K2_LDS, stream>>>(Tf, Xf, (char*)d_out);
}
```

```cpp
#include <hip/hip_runtime.h>
#include <hip/hip_bf16.h>

typedef __attribute__((ext_vector_type(8))) __bf16 bf16x8;
typedef __attribute__((ext_vector_type(4))) short s16x4;
typedef __attribute__((ext_vector_type(8))) short s16x8;
typedef __attribute__((ext_vector_type(16))) float f32x16;
typedef __attribute__((ext_vector_type(4))) float f32x4;
typedef __attribute__((ext_vector_type(4))) unsigned u32x4;
typedef __attribute__((ext_vector_type(2))) unsigned u32x2;
typedef __attribute__((ext_vector_type(16))) unsigned u32x16;
typedef __attribute__((ext_vector_type(2))) __bf16 bf16x2;
typedef __attribute__((ext_vector_type(2))) float f32x2;
typedef unsigned long long u64;
typedef __amdgpu_buffer_rsrc_t rsrc_t;
#define LAS __attribute__((address_space(3)))
#define GAS __attribute__((address_space(1)))

constexpr int D_ = 512, F_ = 1376;
constexpr int NCB = 43;
constexpr int EPO_OFF = 0;
constexpr int FLAG_OFF = 128;
__device__ __forceinline__ int foff(int f) { return f * 128; }
constexpr int TF_OFF = 1 << 20;
constexpr int TF_HALF = NCB * 32 * 1024;
constexpr int XF_OFF = TF_OFF + 2 * TF_HALF;
constexpr int WS_BYTES = XF_OFF + 64 * 32 * 1024;
constexpr int SC1 = 16;

#define BAR() do { __builtin_amdgcn_s_waitcnt(0xC07F); asm volatile("s_barrier" ::: "memory"); SB(); } while (0)
#define BAR0() asm volatile("s_barrier" ::: "memory")
#define WAIT_V(n) asm volatile("s_waitcnt vmcnt(%0)" ::"n"(n) : "memory")
#define SB() __builtin_amdgcn_sched_barrier(0)
#define RSRC(p, n) __builtin_amdgcn_make_buffer_rsrc((void*)(p), 0, (n), 0x00020000)
#define DMA(rs, dst, vo, so, aux) __builtin_amdgcn_raw_ptr_buffer_load_lds(rs, (LAS void*)(dst), 16, vo, so, 0, aux)
#define AUXV 0
#define AUXQ 0
#define AUXX 0
#define AUXU 0
#define AUXY 16
#define PFL 207
#define LD(rs, vo, so, aux) __builtin_amdgcn_raw_buffer_load_b128(rs, vo, so, aux)
#define ST(v, rs, vo, so, aux) __builtin_amdgcn_raw_buffer_store_b128(v, rs, vo, so, aux)
#define MFMA(a, b, c) __builtin_amdgcn_mfma_f32_32x32x16_bf16(a, b, c, 0, 0, 0)
#define MFMA16(a, b, c) __builtin_amdgcn_mfma_f32_16x16x32_bf16(a, b, c, 0, 0, 0)

__device__ __forceinline__ unsigned pk2(float a, float b) {
  f32x2 v = {a, b};
  return __builtin_bit_cast(unsigned, __builtin_convertvector(v, bf16x2));
}
__device__ __forceinline__ float bflo(unsigned u) { return __builtin_bit_cast(float, u << 16); }
__device__ __forceinline__ float bfhi(unsigned u) { return __builtin_bit_cast(float, u & 0xffff0000u); }
__device__ __forceinline__ float addf(float a, float b) { float r; asm("v_add_f32 %0, %1, %2" : "=v"(r) : "v"(a), "v"(b)); return r; }
__device__ __forceinline__ float fmaf_(float a, float b, float c) { float r; asm("v_fma_f32 %0, %1, %2, %3" : "=v"(r) : "v"(a), "v"(b), "v"(c)); return r; }
__device__ __forceinline__ bf16x8 trd2(LAS char* p, LAS char* q) {
  s16x4 a = __builtin_amdgcn_ds_read_tr16_b64_v4i16((LAS s16x4*)p), b = __builtin_amdgcn_ds_read_tr16_b64_v4i16((LAS s16x4*)q);
  s16x8 r = __builtin_shufflevector(a, b, 0, 1, 2, 3, 4, 5, 6, 7);
  return __builtin_bit_cast(bf16x8, r);
}
constexpr unsigned MARK = ~0u;
__device__ __forceinline__ u32x4 pub(u32x4 v) {
#pragma unroll
  for (int e = 0; e < 4; ++e) v[e] = min(v[e], MARK - 1);
  return v;
}
__device__ __forceinline__ unsigned max3u(unsigned a, unsigned b, unsigned c) { return max(max(a, b), c); }
__device__ __forceinline__ unsigned maxq(unsigned m, const u32x4& v) { return max3u(max3u(m, v[0], v[1]), v[2], v[3]); }
__device__ __forceinline__ bool anymark(unsigned m) { return __builtin_amdgcn_ballot_w64(m == MARK) != 0; }
__device__ __forceinline__ u32x4 packh(const f32x16& a, int o) {
  u32x4 r = {pk2(a[o], a[o + 1]), pk2(a[o + 2], a[o + 3]), pk2(a[o + 4], a[o + 5]), pk2(a[o + 6], a[o + 7])};
  return r;
}
#define AUXT2 SC1
#define AUXX2 SC1
#define XSPLIT 1
#define PRIO 0
#define XPRIO 2
#define LHI 1
#define ORD 1
#define X3S 4
#define MPRIO 0
#define RD 5
#define RG 3
#define X3P 2
#define TSM 8
#define XSM 5
#define XMK 0
#define MDL 10
#define XPRE 8
#define XSA 1
#define ROT 20
#define XROT 20
#define RA 6
#define RB 3
#define AUXM SC1
#define TW 2
#define XGO 24
#define IORD 0
#define RSL 0
#define PSL 1
__device__ __forceinline__ u32x4 packq(const f32x16& a, int o) {
  u32x4 r = {pk2(a[o], a[o + 1]), pk2(a[o + 2], a[o + 3]), pk2(a[o + 8], a[o + 9]), pk2(a[o + 10], a[o + 11])};
  return r;
}
template <int I> struct IC { static constexpr int v = I; };
template <int A, int B, class F>
__device__ __forceinline__ void sfor(F&& f) {
  if constexpr (A < B) { f(IC<A>{}); sfor<A + 1, B>(f); }
}
__device__ __forceinline__ void wait_flags(char* ws, int b, u64 want, bool active) {
  if (active) {
    GAS u64* f = (GAS u64*)(ws + FLAG_OFF + foff(b));
#pragma unroll 1
    for (int spin = 0; spin < (1 << 16); ++spin) {
      if (__hip_atomic_load(f, __ATOMIC_RELAXED, __HIP_MEMORY_SCOPE_AGENT) == want) break;
      __builtin_amdgcn_s_sleep(PSL);
    }
  }
  asm volatile("" ::: "memory");
}

#define LA 5
#define LAF 1
#define PKF 0
#define LPR 0
#define DEN 2
constexpr float QUP = DEN ? 0x1p109f : 1.f, QDN = DEN ? 0x1p-40f : 1.f;
constexpr unsigned QSEL = DEN ? 0x5380u : 0x3f80u;
__device__ __forceinline__ float code(unsigned q) { return DEN ? __builtin_bit_cast(float, q) : (float)(int)q; }
#define PFD 0
#define PF1 2
#define XFD 0
#define XF1 2
#define PFA ""
#define TOUCH(a) asm volatile("global_load_dword v255, %0, off " PFA ::"v"(a) : "v255")
#define TL 0
#define XL 0
#define NTX 0
#define NTQ 0
#define NTV 0
#define TP 2
#define XA 4
#define XB 2
#define XC 0
#define XEN 0
#define XED 0
#define XEL 4
#define KR 0
#define YL 1
#define RB1 0
#define TD 2
#define HSL 2
#define HO 1
#define PD 3
constexpr int TNS = TD == 2 ? 4 : 8;
static_assert(TNS >= TD + 2, "ring");
constexpr int T_WQ = TNS * 16384;
constexpr int T_SC = T_WQ + 16384;
constexpr int Y_SCR = 96 * 1024;
constexpr int L_EPO = 153600;
constexpr int L_RDY = L_EPO + 16;
constexpr int LDS_BYTES = L_EPO + 80;
static_assert(T_SC + 704 * 8 <= L_EPO, "lds");

constexpr int SCOL(int st) {
  constexpr int SC[3][12] = {{0, 0, 0, 0, 1, 1, 1, 1, 2, 2, 2, 2}, {0, 1, 2, 0, 1, 2, 0, 0, 1, 1, 2, 2}, {0, 1, 2, 0, 1, 2, 0, 1, 0, 2, 1, 2}};
  return SC[ORD][st];
}
constexpr int SGRP(int st) {
  constexpr int SJ[3][12] = {{0, 1, 2, 3, 0, 1, 2, 3, 0, 1, 2, 3}, {0, 0, 0, 1, 1, 1, 2, 3, 2, 3, 2, 3}, {0, 0, 0, 1, 1, 1, 2, 2, 3, 2, 3, 3}};
  return SJ[ORD][st];
}

#define XROTF (XROT == 20 ? XA * ((idx ^ idx >> 2) & 1) + XB * ((xcd ^ xcd >> 1) & 1) + XC * (xcd >> 2) : XROT == 8 ? 2 * xe + 4 * xs : XROT == 9 ? 2 * xe + xs : XROT == 10 ? 4 * (xe + xs) : XROT == 11 ? 2 * (xe + xs) : XROT == 12 ? (0x630630630630ull >> 4 * ((xe + xs) & 7)) & 7 : XROT == 2 ? 2 * (idx & 3) + (idx & 4) : XROT == 5 ? 2 * (idx & 3) + (idx & 4) + 2 * (xcd & 1) : XROT == 6 ? 4 * ((idx ^ (idx >> 2)) & 1) + 2 * (xcd & 1) : XROT == 7 ? 2 * (idx & 3) + 4 * (xcd & 1) : XROT == 1 ? 4 * ((idx ^ (idx >> 2)) & 1) : XROT == 3 ? ((idx ^ (idx >> 2)) & 1) : XROT == 4 ? 2 * ((idx ^ (idx >> 2)) & 1) : 0)
#define ARG(i) ((char*)(((u64)ka[2 * (i) + 1] << 32) | ka[2 * (i)]))
#define ARGS_HERE() do { asm volatile("" ::"s"(ka)); SB(); } while (0)

__global__ __launch_bounds__(512, 2) void fused(const char* x_, const char* qw_, const char* sc_, const char* ze_,
                                                const char* U_, const char* V_, char* y_, char* ws_) {
  extern __shared__ __attribute__((aligned(1024))) char shm_[];
  LAS char* shm = (LAS char*)shm_;
  u32x16 ka = *(const __attribute__((address_space(4))) u32x16*)__builtin_amdgcn_kernarg_segment_ptr();
  SB();
  const int tid = threadIdx.x, lane = tid & 63;
  const int wv = __builtin_amdgcn_readfirstlane(tid >> 6);
  const int xcd = blockIdx.x & 7, idx = blockIdx.x >> 3;
  if (PFL > 0 && wv == 2) {
    u64 pc;
    asm volatile("s_getpc_b64 %0" : "=s"(pc));
    asm volatile("global_load_dword v255, %0, %1" ::"v"(min((idx & 3) * 64 + lane, PFL - 1) * 128), "s"(pc & ~255ull) : "v255");
  }
  const int h = lane >> 5, r31 = lane & 31, l4 = lane >> 4, l15 = lane & 15;
  const int q = (lane & 15) >> 2, p = lane & 3, blk = (lane >> 4) & 1;
  char* const ws = ARG(7);
  auto raise_flag = [&](int f) {
    if (tid == 0) __hip_atomic_store((GAS u64*)(ws + FLAG_OFF + foff(f)), *(LAS u64*)(shm + L_EPO), __ATOMIC_RELAXED, __HIP_MEMORY_SCOPE_AGENT);
    SB();
  };
  if (__builtin_expect(idx >= 30, 0)) {
    const int fid = 240 + xcd * 2 + (idx & 1);
    ARGS_HERE();
    const rsrc_t rM = RSRC(ws + TF_OFF + (fid - 240) * 172 * 1024, 172 * 1024), rN = RSRC(ws + XF_OFF + (fid - 240) * 131072, 131072);
    const u32x4 mark = {MARK, MARK, MARK, MARK};
    static_assert(2 * TF_HALF == 16 * 172 * 1024, "markers");
    u64 e = 0;
    if (tid == 0) e = __hip_atomic_load((GAS u64*)(ws + EPO_OFF), __ATOMIC_RELAXED, __HIP_MEMORY_SCOPE_AGENT);
    SB();
    if (MDL) __builtin_amdgcn_s_sleep(MDL);
#pragma unroll
    for (int i = 0; i < 4 * XMK; ++i) ST(mark, rN, (tid >> 7) * 8192 + (tid & 127) * 16 + (i >> 2) * 2048, (i & 3) * 32768, AUXM);
#pragma unroll
    for (int i = 0; i < 6; ++i) ST(mark, rM, tid * 16 + i * 8192, 0, AUXM);
    SB();
    WAIT_V(6);
    if (tid == 0) *(LAS u64*)(shm + L_EPO) = e + 1;
    BAR();
    raise_flag(fid + 16);
#pragma unroll 2
    for (int i = 6; i < 22; ++i) ST(mark, rM, tid * 16 + i * 8192, 0, AUXM);
    WAIT_V(0);
    BAR();
    raise_flag(fid);
    const u64 want = *(LAS u64*)(shm + L_EPO);
    if (blockIdx.x == 255 && wv == 4) {
#pragma unroll 1
      for (int i = 0; i < 4; ++i) wait_flags(ws, i * 64 + lane, want, true);
      if (lane == 0) __hip_atomic_store((GAS u64*)(ws + EPO_OFF), want, __ATOMIC_RELAXED, __HIP_MEMORY_SCOPE_AGENT);
    }
    return;
  }
  const bool isX = idx < 8;
  int notX = idx >> 3;
  asm volatile("" : "+s"(notX));
  const int bn = (TL == 0 ? xcd >> 2 : TL == 2 ? xcd >> 1 : 0) * 22 + idx - 8;
  const int kh = xcd & 1, mg = TL == 0 ? 2 * (xcd & 2) + (bn & 3) : TL == 1 ? (xcd & 6) + (bn & 1) : bn & 7, ng = bn >> (TL == 0 ? 2 : TL == 1 ? 1 : 3), n0 = min(ng * 128, F_ - 128);
  const int xs = XL ? xcd * 2 + (idx & 1) : (xcd >> 1) * 4 + (idx & 3), xe = XL ? idx >> 1 : (xcd & 1) * 2 + (idx >> 2);
  const int yl = (YL == 2 ? 0 : YL & 1 ? xcd & 1 : xcd >> 1) * 30 + idx, yg = yl >> (YL == 2 ? 1 : YL & 1 ? 2 : 3), stile = YL == 2 ? xcd * 2 + (yl & 1) : YL & 1 ? ((xcd >> 1) ^ (YL >> 1)) * 4 + (yl & 3) : (xcd & 1) * 8 + (yl & 7), cb0 = min(yg * 3, NCB - 3);
  const int krot = KR >> 4 * (idx >> 3) & 3;
  auto kr = [&](int j) { return ((j + krot) & 3) * 2048; };
  LAS unsigned* const cnt = (LAS unsigned*)(shm + L_EPO + 8);
  auto lds_wait = [&](LAS unsigned* p, unsigned mask, unsigned v) {
#pragma unroll 1
    for (int spin = 0; spin < (1 << 20); ++spin) {
      if ((__builtin_amdgcn_readfirstlane(*(volatile LAS unsigned*)p) & mask) == v) break;
    }
    asm volatile("" ::: "memory");
  };

  if (__builtin_expect(wv >= 4, 1)) {
   const int lw = wv - 4;
   u32x4 xa[32];
   const int sXF = XF_OFF + (stile * 4 + lw) * 32768, xflag = 176 + stile * 4 + (lane & 3);
   auto xldr = [&](rsrc_t r, int j, int g) {
     xa[8 * (g >> 1) + 2 * j + (g & 1)] = LD(r, lane * 16 + (g & 1) * 1024, sXF + (g >> 1) * 8192 + kr(j), AUXX2);
     SB();
   };
   auto xpeek = [&]() {
     const u64 v = __hip_atomic_load((GAS u64*)(ws + FLAG_OFF + foff(xflag)), __ATOMIC_RELAXED, __HIP_MEMORY_SCOPE_AGENT);
     SB();
     return v;
   };
   int xok = 0;
   u64 early = 0;
   constexpr int XE0 = 44 - LA + XED, NXE = XEN * (44 - XE0);
   if (isX) {
    const int l3 = lane >> 3;
    int uo[4], xo[4];
#pragma unroll
    for (int j = 0; j < 4; ++j) {
      uo[j] = (4 * j + l4) * (D_ * 2) + 16 * (l15 ^ ((l4 << 2) | j));
      xo[j] = (8 * j + l3) * (D_ * 2) + 16 * ((lane & 7) ^ (4 * (j & 1) + (l3 >> 1)));
    }
    const int sU = 16 * lw * (D_ * 2) + xe * 256, sX = (xs * 128 + 32 * lw) * (D_ * 2);
#pragma unroll
    for (int j = 0; j < 4; ++j) asm volatile("" : "+v"(uo[j]), "+v"(xo[j]));
    ARGS_HERE();
    const rsrc_t rU = RSRC(ARG(4), D_ * D_ * 2), rX = RSRC(ARG(0), 2048 * D_ * 2);
#pragma unroll 1
    for (int u = 0; u < 8; ++u) {
      const int st = (XROTF + u) & 7;
      if (u < NTX) {
#pragma unroll
        for (int j = 0; j < 4; ++j) {
          DMA(rU, shm + (u & 3) * 32768 + (4 * lw + j) * 1024, uo[j], sU + st * 64 * (D_ * 2), AUXU);
          DMA(rX, shm + (u & 3) * 32768 + 16384 + (4 * lw + j) * 1024, xo[j], sX + st * 128, 2);
        }
      } else {
#pragma unroll
        for (int j = 0; j < 4; ++j) {
          DMA(rU, shm + (u & 3) * 32768 + (4 * lw + j) * 1024, uo[j], sU + st * 64 * (D_ * 2), AUXU);
          DMA(rX, shm + (u & 3) * 32768 + 16384 + (4 * lw + j) * 1024, xo[j], sX + st * 128, AUXX);
        }
      }
      SB();
      if (u > 0) {
        WAIT_V(8);
        BAR0();
      }
    }
    WAIT_V(0);
    BAR0();
#pragma unroll
    for (int i = 0; i < NXE; ++i) asm volatile("" : "=v"(xa[8 * ((i & 7) >> 1) + 2 * (i >> 3) + (i & 1)]));
   }
   if (notX) {
    const int k0 = kh * 672;
    const int s0 = ROT == 20 ? RA * ((mg + ng) % TP) + RB * ((xcd >> 1 ^ xcd >> 2) & 1) : ROT == 11 ? (0x8630 >> 4 * ((mg + ng) & 3)) & 15 : ROT == 12 ? (0x8630 >> 4 * ((mg + ng + (mg >> 2) * RA) & 3)) & 15 : ROT == 10 ? (RA * mg + RB * ng) % 11 : ROT == 3 ? (int)(0x924924924ull * 4 >> 2 * (mg + ng)) & 12 : ROT == 2 ? 5 * ((mg + ng) & 1) : ROT == 4 ? (int)(0x924924924ull * 2 >> 2 * (mg + ng)) & 6 : ROT == 5 ? 2 * ((mg + ng) & 1) : 0;
    int vo[4], qo[4];
#pragma unroll
    for (int j = 0; j < 4; ++j) {
      vo[j] = (4 * j + l4) * (F_ * 2) + 16 * (l15 ^ ((l4 << 2) | j));
      qo[j] = (4 * j + l4) * (F_ * 4) + l15 * 16;
    }
    const int t = min(lw * 22 + lane, 87);
    int to = t * 16, fo = l15 * 8;
    const int sV = (k0 + 16 * lw) * (F_ * 2) + n0 * 2, sQ = (mg * 64 + 16 * lw) * (F_ * 4) + k0 * 4, sS = ((mg >> 1) * F_ + k0) * 2;
#pragma unroll
    for (int j = 0; j < 4; ++j) asm volatile("" : "+v"(vo[j]), "+v"(qo[j]));
    asm volatile("" : "+v"(to), "+v"(fo));
    ARGS_HERE();
    const rsrc_t rV = RSRC(ARG(5), F_ * F_ * 2), rQ = RSRC(ARG(1), D_ * F_ * 4), rS = RSRC(ARG(2), 4 * F_ * 2), rZ = RSRC(ARG(3), 4 * F_ * 2);
    u32x4 qr[TD + 1][4];
    auto issue = [&](int u, int j) {
      const int st = s0 + u >= 11 ? s0 + u - 11 : s0 + u;
      if (u < NTV) DMA(rV, shm + (u % TNS) * 16384 + (4 * lw + j) * 1024, vo[j], sV + st * 64 * (F_ * 2), 2);
      else DMA(rV, shm + (u % TNS) * 16384 + (4 * lw + j) * 1024, vo[j], sV + st * 64 * (F_ * 2), AUXV);
      SB();
      qr[u % (TD + 1)][j] = u < NTQ ? LD(rQ, qo[j], sQ + st * 256, 2) : LD(rQ, qo[j], sQ + st * 256, AUXQ);
      SB();
    };
    if (LPR) __builtin_amdgcn_s_setprio(LPR);
    issue(0, 0);
    u32x2 sf = __builtin_amdgcn_raw_buffer_load_b64(rS, fo, sS + s0 * 128, 0), zf = __builtin_amdgcn_raw_buffer_load_b64(rZ, fo, sS + s0 * 128, 0);
    u32x4 sv = LD(rS, to, sS, 0), zv = LD(rZ, to, sS, 0);
    SB();
#pragma unroll
    for (int i = 1; i < LA; ++i) issue(i >> 2, i & 3);
    int wo[4];
#pragma unroll
    for (int j = 0; j < 4; ++j) {
      const int row = 16 * lw + 4 * j + l4;
      wo[j] = T_WQ + 128 * row + 16 * ((l15 >> 1) ^ ((row >> 1) & 7)) + 8 * (lane & 1);
    }
    auto table = [&]() {
      if (kh == 1 && t < 4) { sv = u32x4{0, 0, 0, 0}; zv = sv; }
      f32x4 c0 = {bflo(sv[0]), bfhi(sv[0]), bflo(sv[1]), bfhi(sv[1])}, c1 = {bflo(sv[2]), bfhi(sv[2]), bflo(sv[3]), bfhi(sv[3])};
      f32x4 z0 = {bflo(zv[0]), bfhi(zv[0]), bflo(zv[1]), bfhi(zv[1])}, z1 = {bflo(zv[2]), bfhi(zv[2]), bflo(zv[3]), bfhi(zv[3])};
      int o = t - 8 * s0;
      o = T_SC + (o < 0 ? o + 88 : o) * 32;
      if (lane < 22) {
        *(LAS f32x4*)(shm + o) = c0 * QUP;
        *(LAS f32x4*)(shm + o + 16) = c1 * QUP;
        *(LAS f32x4*)(shm + o + 2816) = z0 * c0 * -QDN;
        *(LAS f32x4*)(shm + o + 2816 + 16) = z1 * c1 * -QDN;
      }
      SB();
    };
    f32x4 s[2], m[2];
    if (kh == 1 && s0 == 0 && l15 < 8) { sf = u32x2{0, 0}; zf = sf; }
    s[0] = f32x4{bflo(sf[0]), bfhi(sf[0]), bflo(sf[1]), bfhi(sf[1])};
    m[0] = f32x4{bflo(zf[0]), bfhi(zf[0]), bflo(zf[1]), bfhi(zf[1])} * s[0] * -QDN;
    s[0] *= QUP;
#pragma unroll
    for (int u = 0; u < 11; ++u) {
#pragma unroll
      for (int j = 0; j < 4; ++j) {
        if (!LAF && 4 * u + j + LA < 44) issue((4 * u + j + LA) >> 2, (j + LA) & 3);
        if (u == 0 && j == 1) table();
        if (j == (u == 0 ? 4 : 0) && u < 2) {
          s[1] = *(LAS f32x4*)(shm + T_SC + 256 + l15 * 16);
          m[1] = *(LAS f32x4*)(shm + T_SC + 2816 + 256 + l15 * 16);
        }
        if (j == 2 && u > 0 && u < 10) {
          s[(u + 1) & 1] = *(LAS f32x4*)(shm + T_SC + (u + 1) * 256 + l15 * 16);
          m[(u + 1) & 1] = *(LAS f32x4*)(shm + T_SC + 2816 + (u + 1) * 256 + l15 * 16);
        }
        const u32x4 v = qr[u % (TD + 1)][j];
        const f32x4 cs = s[u & 1], cm = m[u & 1];
        u32x2 o;
        if (PKF) {
          f32x2 lo = {code(v[0]), code(v[1])}, hi = {code(v[2]), code(v[3])};
          lo = __builtin_elementwise_fma(lo, f32x2{cs[0], cs[1]}, f32x2{cm[0], cm[1]});
          hi = __builtin_elementwise_fma(hi, f32x2{cs[2], cs[3]}, f32x2{cm[2], cm[3]});
          o = u32x2{pk2(lo[0], lo[1]), pk2(hi[0], hi[1])};
        } else if (DEN == 2) {
          unsigned o0, o1, t0, t1;
          asm("v_fma_f32 %0, %4, %8, %12\n\tv_fma_f32 %2, %5, %9, %13\n\tv_fma_f32 %1, %6, %10, %14\n\tv_fma_f32 %3, %7, %11, %15\n\t"
              "v_cvt_pk_bf16_f32 %0, %0, %2\n\tv_cvt_pk_bf16_f32 %1, %1, %3"
              : "=&v"(o0), "=&v"(o1), "=&v"(t0), "=&v"(t1)
              : "v"(v[0]), "v"(v[1]), "v"(v[2]), "v"(v[3]), "v"(cs[0]), "v"(cs[1]), "v"(cs[2]), "v"(cs[3]), "v"(cm[0]), "v"(cm[1]), "v"(cm[2]), "v"(cm[3]));
          o = u32x2{o0, o1};
        } else {
          o = u32x2{pk2(fmaf_(code(v[0]), cs[0], cm[0]), fmaf_(code(v[1]), cs[1], cm[1])),
                    pk2(fmaf_(code(v[2]), cs[2], cm[2]), fmaf_(code(v[3]), cs[3], cm[3]))};
        }
        *(LAS u32x2*)(shm + wo[j] + (u & 1) * 8192) = o;
        SB();
        if (LAF && 4 * u + j + LA < 44) issue((4 * u + j + LA) >> 2, (j + LA) & 3);
        if (XEN && 4 * u + j == XE0 - XEL) early = xpeek();
        if (XEN && 4 * u + j == XE0) {
          xok = __builtin_amdgcn_ballot_w64(early != *(LAS u64*)(shm + L_EPO)) ? 0 : WS_BYTES;
          early = xpeek();
        }
        if (XEN && 4 * u + j >= XE0) {
#pragma unroll
          for (int i = XEN * (4 * u + j - XE0); i < XEN * (4 * u + j + 1 - XE0); ++i) xldr(RSRC(ws, xok), i >> 3, i & 7);
        }
      }
      BAR();
    }
   }
    const rsrc_t rW = RSRC(ws, WS_BYTES);
    const u64 want = *(LAS u64*)(shm + L_EPO);
    const int w = wv - 4;
    if (XPRIO) __builtin_amdgcn_s_setprio(XPRIO);
    auto xld = [&](int j, int g) { xldr(rW, j, g); };
    auto xmax = [&](unsigned m, int j, int g) { return maxq(m, xa[8 * (g >> 1) + 2 * j + (g & 1)]); };
    auto xfix = [&](unsigned m, int j) {
      if (__builtin_expect(anymark(m), 0)) {
#pragma unroll 1
        for (int spin = 0; spin < (1 << 14); ++spin) {
          asm volatile("" ::: "memory");
#pragma unroll
          for (int g = 0; g < 8; ++g) xld(j, g);
          m = 0;
#pragma unroll
          for (int g = 0; g < 8; ++g) m = xmax(m, j, g);
          if (!anymark(m)) break;
        }
      }
      SB();
    };
    auto xchk = [&](int j) {
      unsigned m = 0;
#pragma unroll
      for (int g = 0; g < 8; ++g) m = xmax(m, j, g);
      xfix(m, j);
    };
    const int go = L_RDY + 48 + w;
    constexpr int NXA = X3S < 0 ? 4 : 3;
    if (isX) lds_wait(cnt, 0xff00u, 0x400u);
    if (isX || !XEN) early = want - 1;
    if (XSA && XMK) early = xpeek();
    if (XEN && !xok) {
      wait_flags(ws, xflag, want, lane < 4 && early != want);
#pragma unroll
      for (int i = 0; i < NXE; ++i) xld(i >> 3, i & 7);
    }
#pragma unroll
    for (int j = 0; j < NXA; ++j) {
      if (j == XMK && !XEN) wait_flags(ws, xflag, want, lane < 4 && early != want);
#pragma unroll
      for (int g = 0; g < 8; ++g) {
        if (8 * j + g == XPRE && XSPLIT) lds_wait(cnt, 255u, 4u);
        if (8 * j + g >= NXE) xld(j, g);
        if (8 * j + g + 1 == XGO) { asm volatile("s_mov_b64 exec, 1\n\tds_write_b8 %0, %1\n\ts_mov_b64 exec, -1" ::"v"(go), "v"(1) : "memory"); SB(); }
      }
    }
#pragma unroll
    for (int j = 0; j < XMK; ++j) xchk(j);
    if (XPRIO != MPRIO) __builtin_amdgcn_s_setprio(MPRIO);
    LAS char* scr = shm + Y_SCR + w * 2560;
    int one = 1;
    asm volatile("" : "+v"(one));
    const int yo = (lane >> 2) * (F_ * 2) + (lane & 3) * 16, sY = (stile * 128 + w * 32) * (F_ * 2) + cb0 * 64;
    f32x16 acc[3];
    bf16x8 t[8];
    auto put = [&](int c, int i) {
      *(LAS u32x4*)(scr + (HO && c < 2 ? (c + 1) * 10240 : 0) + 80 * r31 + 32 * h + 16 * i) = packh(acc[c], 8 * i);
      SB();
      if (HO && c < 2 && i) { asm volatile("s_mov_b64 exec, 1\n\tds_write_b8 %0, %1 offset:%2\n\ts_mov_b64 exec, -1" ::"v"(go), "v"(one), "n"(4 + 4 * (c & 1)) : "memory"); SB(); }
    };
    u32x4 yv[2];
    auto get = [&](int i) {
      yv[i] = *(LAS u32x4*)(scr + 80 * (lane >> 2) + 16 * (lane & 3) + 1280 * i);
      SB();
    };
    auto out = [&](int c, int i) {
      const rsrc_t rY = RSRC(ARG(6), cb0 + c >= yg * 3 ? 2048 * F_ * 2 : 0);
      ST(yv[i], rY, yo + 16 * i * (F_ * 2), sY + c * 64, AUXY);
      SB();
    };
    int to = lane * 16, to2 = lane * 16 + 65536, ro = L_RDY;
    asm volatile("" : "+v"(to2), "+v"(ro));
    unsigned rdy = 0;
    sfor<-RD, 96>([&](auto ic) __attribute__((always_inline)) {
      constexpr int m = decltype(ic)::v;
      if constexpr (m >= 0 && !RB1) {
        constexpr int st = m >> 3, g = m & 7, c = SCOL(st), ks = 8 * (g >> 1) + 2 * SGRP(st) + (g & 1);
        if (SGRP(st) == 0 && g == 0) acc[c] = MFMA(t[g], __builtin_bit_cast(bf16x8, xa[ks]), f32x16{});
        else acc[c] = MFMA(t[g], __builtin_bit_cast(bf16x8, xa[ks]), acc[c]);
        SB();
      }
      if constexpr (m + RD < 96) {
        constexpr int st = (m + RD) >> 3, g = (m + RD) & 7;
        if (g == 0) {
          unsigned sr, sn, vv;
          asm volatile("s_cmp_eq_u32 %3, 0x1010101\n\ts_cbranch_scc1 .Lrdy%=\n\ts_mov_b32 %1, 0x100000\n"
                       ".Lspin%=:\n\tds_read_b32 %2, %4 offset:%5\n\ts_waitcnt lgkmcnt(0)\n\tv_readfirstlane_b32 %0, %2\n\t"
                       "s_cmp_eq_u32 %0, 0x1010101\n\ts_cbranch_scc1 .Lrdy%=\n\ts_sub_u32 %1, %1, 1\n\ts_cbranch_scc0 .Lspin%=\n.Lrdy%=:"
                       : "=&s"(sr), "=&s"(sn), "=&v"(vv) : "s"(__builtin_amdgcn_readfirstlane(rdy)), "v"(ro), "n"(4 * st) : "memory", "scc");
          SB();
        }
        t[g] = *(LAS bf16x8*)(shm + (SCOL(st) < 2 ? to : to2) + (SCOL(st) & 1) * 32768 + (8 * (g >> 1) + 2 * SGRP(st) + (g & 1)) * 1024);
        SB();
        if (g == 8 - RG && st < 11) { rdy = *(volatile LAS unsigned*)(shm + ro + 4 * (st + 1)); SB(); }
      }
      if constexpr (m >= 0 && RB1) {
        constexpr int st = m >> 3, g = m & 7, c = SCOL(st), ks = 8 * (g >> 1) + 2 * SGRP(st) + (g & 1);
        if (SGRP(st) == 0 && g == 0) acc[c] = MFMA(t[g], __builtin_bit_cast(bf16x8, xa[ks]), f32x16{});
        else acc[c] = MFMA(t[g], __builtin_bit_cast(bf16x8, xa[ks]), acc[c]);
        SB();
      }
      if (X3S >= 0 && m >= X3S && m < X3S + 8 * X3P && (m - X3S) % X3P == 0) {
        if (XPRIO != MPRIO) __builtin_amdgcn_s_setprio(XPRIO);
        xld(3, (m - X3S) / X3P);
        if (XPRIO != MPRIO) __builtin_amdgcn_s_setprio(MPRIO);
      }
      if constexpr (m >= 8) {
        constexpr int st = (m >> 3) - 1, g = m & 7;
        if (SGRP(st) == 3 && (g == 1 || g == 2)) put(SCOL(st), g - 1);
        if (SGRP(st) == 3 && (g == 5 || g == 6) && !HO) get(g - 5);
      }
      if constexpr (m >= 16) {
        constexpr int st = (m >> 3) - 2, g = m & 7;
        if (SGRP(st) == 3 && (g == 2 || g == 3) && !HO) out(SCOL(st), g - 2);
      }
    });
    if (!HO && SGRP(10) == 3) { out(SCOL(10), 0); out(SCOL(10), 1); }
    put(2, 0);
    put(2, 1);
    get(0);
    get(1);
    out(2, 0);
    out(2, 1);
  } else {
   ARGS_HERE();
   const rsrc_t rW = RSRC(ws, WS_BYTES);
   const int fid = isX ? 176 + xs * 4 + xe : kh * 88 + ng * 8 + mg;
   auto nroff = [&](int r, int ks) { return 128 * r + 16 * ((2 * ks + h) ^ ((r >> 1) & 7)); };
   u64 seen = 0;
   if (wv == 3) seen = __hip_atomic_load((GAS u64*)(ws + EPO_OFF), __ATOMIC_RELAXED, __HIP_MEMORY_SCOPE_AGENT);
   auto epoch_to_lds = [&]() {
     if (wv == 3 && lane < 10) *(LAS u64*)(shm + L_EPO + lane * 8) = lane ? 0 : seen + 1;
     SB();
   };
   auto sample = [&](int f) {
     if (wv == 1) seen = __hip_atomic_load((GAS u64*)(ws + FLAG_OFF + foff(f + l15)), __ATOMIC_RELAXED, __HIP_MEMORY_SCOPE_AGENT);
     SB();
   };
   auto confirm = [&](int f) {
     if (wv == 1) {
       const u64 want = *(LAS u64*)(shm + L_EPO);
       if (__builtin_expect(__builtin_amdgcn_ballot_w64(seen != want) != 0, 0)) wait_flags(ws, f + l15, want, true);
     }
     SB();
   };
   if (isX) {
    const int eh = wv & 1, sh = wv >> 1;
    const int xr0 = XROTF;
    const u64 pa = (u64)(wv ? ARG(4) + ((idx & 3) * 16 + (lane >> 1 & 15)) * (D_ * 2) + xe * 256 + (lane & 1) * 128 : ARG(0) + (xs * 128 + (idx & 4) * 16 + lane) * (D_ * 2));
    const int ps = wv ? 64 * D_ * 2 : 128;
    auto touch = [&](int st) {
      if (XFD && st < 8 && !(wv & 1)) TOUCH(pa + ((xr0 + st) & 7) * ps);
      SB();
    };
#pragma unroll
    for (int st = XF1; st <= XFD; ++st) touch(st);
    int xo[2][4], uo[2][2];
#pragma unroll
    for (int j = 0; j < 2; ++j)
#pragma unroll
      for (int ks = 0; ks < 4; ++ks) xo[j][ks] = 16384 + nroff(64 * sh + 32 * j + r31, ks);
#pragma unroll
    for (int c = 0; c < 2; ++c)
#pragma unroll
      for (int t = 0; t < 2; ++t)
        uo[c][t] = 256 * (8 * h + 4 * t + q) + 16 * ((((2 * eh + c) ^ q) << 2) | ((2 * blk + (p >> 1)) ^ (2 * h + t))) + 8 * (p & 1);
    const int so = XF_OFF + (xs * 4 + 2 * sh) * 32768 + (xe * 4 + 2 * eh) * 2048;
    int xo2[2][4], uo2[2][2];
#pragma unroll
    for (int j = 0; j < 2; ++j) {
#pragma unroll
      for (int ks = 0; ks < 4; ++ks) { xo2[j][ks] = xo[j][ks] + 65536; asm volatile("" : "+v"(xo2[j][ks])); }
#pragma unroll
      for (int t = 0; t < 2; ++t) { uo2[j][t] = uo[j][t] + 65536; asm volatile("" : "+v"(uo2[j][t])); }
    }
    f32x16 acc[2][2] = {};
    bf16x8 a[2][4][2], b[2][4][2];
#pragma unroll
    for (int st = 0; st < 8; ++st) {
      if (st == 2) epoch_to_lds();
      if (XMK && st == 7) confirm(256);
      BAR();
      touch(st + XFD + 1);
      auto rd = [&](int ks, int c) {
        const int u0 = (st & 2) ? uo2[c][0] : uo[c][0], u1 = (st & 2) ? uo2[c][1] : uo[c][1], x0 = (st & 2) ? xo2[c][ks] : xo[c][ks];
        a[st & 1][ks][c] = trd2(shm + u0 + (st & 1) * 32768 + ks * 4096, shm + u1 + (st & 1) * 32768 + ks * 4096);
        b[st & 1][ks][c] = *(LAS bf16x8*)(shm + x0 + (st & 1) * 32768);
        SB();
      };
#pragma unroll
      for (int i = 0; i < 4; ++i) {
        if (st > 0) { acc[i >> 1][i & 1] = MFMA(a[(st - 1) & 1][3][i >> 1], b[(st - 1) & 1][3][i & 1], acc[i >> 1][i & 1]); SB(); }
        rd(i >> 1, i & 1);
      }
#pragma unroll
      for (int i = 0; i < 4; ++i) {
        acc[i >> 1][i & 1] = MFMA(a[st & 1][0][i >> 1], b[st & 1][0][i & 1], acc[i >> 1][i & 1]);
        SB();
        rd(2 + (i >> 1), i & 1);
        if (XMK && st == XSM && i == 3) sample(256);
      }
      if (st < 7) {
#pragma unroll
        for (int ks = 1; ks < 3; ++ks)
#pragma unroll
          for (int i = 0; i < 4; ++i) acc[i >> 1][i & 1] = MFMA(a[st & 1][ks][i >> 1], b[st & 1][ks][i & 1], acc[i >> 1][i & 1]);
        SB();
      }
    }
#pragma unroll
    for (int i = 0; i < 5; ++i) {
      if (i < 4) {
#pragma unroll
        for (int ks = 1; ks < 4; ++ks) acc[i >> 1][i & 1] = MFMA(a[1][ks][i >> 1], b[1][ks][i & 1], acc[i >> 1][i & 1]);
        SB();
      }
      if (i > 0) {
        const int c = (i - 1) >> 1, j = (i - 1) & 1;
        ST(XMK ? pub(packh(acc[c][j], 0)) : packh(acc[c][j], 0), rW, lane * 16 + c * 2048, so + j * 32768, SC1);
        ST(XMK ? pub(packh(acc[c][j], 8)) : packh(acc[c][j], 8), rW, lane * 16 + 1024 + c * 2048, so + j * 32768, SC1);
        SB();
      }
    }
   }
   if (notX) {
    const int mh = wv & 1, nh = wv >> 1;
    u64 pa;
    int ps;
    {
      const int ev = min((idx - 8) * 38 + lane, 831), eq = min((idx - 8) * 24 + lane - 38, 511), er = ev / 13, nlo = (xcd >> 2) * 1280;
      pa = (u64)(lane < 38 ? ARG(5) + (kh * 672 + er) * (F_ * 2) + min(nlo + (ev - 13 * er) * 128, xcd >> 2 ? F_ * 2 - 4 : 1532)
                           : ARG(1) + (128 * (xcd & 2) + (eq >> 1)) * (F_ * 4) + kh * 672 * 4 + (eq & 1) * 128);
      ps = lane < 38 ? 64 * F_ * 2 : 256;
      pa += PF1 * ps;
    }
    auto touch = [&](int st) {
      if (PFD && st < 11 && wv == 0) { TOUCH(pa); pa += ps; }
      SB();
    };
#pragma unroll
    for (int st = PF1; st <= PFD; ++st) touch(st);
    int ao[4], bo[2][2];
#pragma unroll
    for (int ks = 0; ks < 4; ++ks) ao[ks] = T_WQ + nroff(32 * mh + r31, ks);
#pragma unroll
    for (int c = 0; c < 2; ++c)
#pragma unroll
      for (int t = 0; t < 2; ++t)
        bo[c][t] = 256 * (8 * h + 4 * t + q) + 16 * ((((2 * nh + c) ^ q) << 2) | ((2 * (p & 1) + blk) ^ (2 * h + t))) + 8 * (p >> 1);
    const int to = (r31 >> 4) * 1024 + h * 256 + l15 * 16;
    const int so = TF_OFF + kh * TF_HALF + (n0 / 32 + 2 * nh) * 32768 + (mg * 2 + mh) * 2048;
    f32x16 acc[2] = {};
#pragma unroll
    for (int st = 0; st < 11; ++st) {
      if (st == 2) epoch_to_lds();
      if (st == 10) confirm(240);
      BAR();
      touch(st + PFD + 1);
      bf16x8 a[4], b[4][2];
      auto rd = [&](int ks) {
        a[ks] = *(LAS bf16x8*)(shm + ao[ks] + (st & 1) * 8192);
#pragma unroll
        for (int c = 0; c < 2; ++c)
          b[ks][c] = trd2(shm + bo[c][0] + (st % TNS) * 16384 + ks * 4096, shm + bo[c][1] + (st % TNS) * 16384 + ks * 4096);
        SB();
      };
      rd(0);
      rd(1);
      rd(2);
      if (st < 10) {
#pragma unroll
        for (int ks = 0; ks < 4; ++ks) {
#pragma unroll
          for (int c = 0; c < 2; ++c) acc[c] = MFMA(a[ks], b[ks][c], acc[c]);
          SB();
          if (ks == 0) rd(3);
          if (ks == 1 && st == TSM) sample(240);
        }
      } else {
        acc[0] = MFMA(a[0], b[0][0], acc[0]);
        SB();
        rd(3);
#pragma unroll
        for (int ks = 1; ks < 4; ++ks) acc[0] = MFMA(a[ks], b[ks][0], acc[0]);
#pragma unroll
        for (int ks = 0; ks < 4; ++ks) acc[1] = MFMA(a[ks], b[ks][1], acc[1]);
        SB();
      }
    }
    if (LHI) __builtin_amdgcn_s_setprio(3);
#pragma unroll
    for (int c = 0; c < 2; ++c) {
      if (n0 / 32 + 2 * nh + c >= ng * 4) {
        ST(pub(packq(acc[c], 0)), rW, to, so + c * 32768, SC1);
        ST(pub(packq(acc[c], 4)), rW, to + 512, so + c * 32768, SC1);
      }
    }
   }
    const u64 want = *(LAS u64*)(shm + L_EPO);
    const int co = L_EPO + 8;
    asm volatile("s_mov_b64 exec, 1\n\tds_add_u32 %0, %1\n\ts_mov_b64 exec, -1" ::"v"(co), "v"(1) : "memory");
    if (isX) {
      unsigned n, sn;
      asm volatile("s_mov_b64 exec, 1\n\ts_waitcnt vmcnt(0)\n\tds_add_rtn_u32 %0, %2, %3\n\ts_waitcnt lgkmcnt(0)\n\t"
                   "v_readfirstlane_b32 %1, %0\n\ts_cmpk_lt_u32 %1, 0x300\n\ts_cbranch_scc1 .Lnf%=\n\t"
                   "global_store_dwordx2 %4, %5, %6 offset:128 sc1\n.Lnf%=:\n\ts_mov_b64 exec, -1"
                   : "=&v"(n), "=&s"(sn) : "v"(co), "v"(256), "v"(foff(fid)), "v"(want), "s"(ws) : "memory", "scc");
    }
    u32x4 sel = {0, 0, 0, 0};
#pragma unroll
    for (int e = 0; e < 4; ++e) sel[e] = ((l15 >> 3) == blk && ((l15 & 7) >> 1) == e) ? ((lane & 1) ? QSEL << 16 : QSEL) : 0u;
    const int sT = TF_OFF + cb0 * 32768 + wv * 8192, to = r31 * 16 + h * TF_HALF;
    int io = wv * 8192 + blk * 1024 + h * 512 + l15 * 16, io2 = io + 65536, ro = L_RDY + wv, one = 1;
    asm volatile("" : "+v"(io), "+v"(io2), "+v"(ro), "+v"(one), "+v"(sel));
    u32x4 tr[PD][4];
    f32x4 d[4];
    auto issue = [&](int st) {
#pragma unroll
      for (int i = 0; i < 4; ++i) tr[st % PD][i] = LD(rW, to + i * 512, sT + SCOL(st) * 32768 + kr(SGRP(st)), AUXT2);
      SB();
    };
    auto missing = [&](int st) { return anymark(maxq(maxq(maxq(maxq(0u, tr[st % PD][0]), tr[st % PD][1]), tr[st % PD][2]), tr[st % PD][3])); };
    auto image = [&](int st) {
#pragma unroll
      for (int i = 0; i < 4; ++i) d[i] = MFMA16(__builtin_bit_cast(bf16x8, sel), __builtin_bit_cast(bf16x8, tr[st % PD][i]), f32x4{});
      SB();
#pragma unroll
      for (int cc = 0; cc < 2; ++cc) {
        const u32x4 o = u32x4{pk2(d[2 * cc][0], d[2 * cc][1]), pk2(d[2 * cc][2], d[2 * cc][3]),
                         pk2(d[2 * cc + 1][0], d[2 * cc + 1][1]), pk2(d[2 * cc + 1][2], d[2 * cc + 1][3])};
        *(LAS u32x4*)(shm + (SCOL(st) < 2 ? io : io2) + (SCOL(st) & 1) * 32768 + SGRP(st) * 2048 + cc * 256) = o;
      }
    };
    if (isX) wait_flags(ws, 240 + l15, want, lane < 16);
    else raise_flag(fid);
    lds_wait(cnt, 255u, 4u);
    lds_wait((LAS unsigned*)(shm + L_RDY + 48), ~0u, 0x01010101u);
#pragma unroll 1
    for (int spin = 0; spin < (1 << 14); ++spin) {
      asm volatile("" ::: "memory");
#pragma unroll
      for (int st = 0; st < TW; ++st) issue(st);
      if (!missing(0)) break;
      if (RSL) __builtin_amdgcn_s_sleep(RSL);
    }
#pragma unroll
    for (int st = TW; st < PD - IORD; ++st) issue(st);
#pragma unroll
    for (int st = 0; st < 12; ++st) {
      image(st);
      if (st > 0 && __builtin_expect(__builtin_amdgcn_ballot_w64(__builtin_isunordered(d[0][0], d[1][0]) | __builtin_isunordered(d[2][0], d[3][0])) != 0, 0)) {
#pragma unroll 1
        for (int spin = 0; spin < (1 << 14) && missing(st); ++spin) {
          asm volatile("" ::: "memory");
#pragma unroll
          for (int u = st; u < st + PD && u < 12; ++u) issue(u);
        }
        image(st);
      }
      asm volatile("s_mov_b64 exec, 1\n\tds_write_b8 %0, %1 offset:%2\n\ts_mov_b64 exec, -1" ::"v"(ro), "v"(one), "n"(4 * st) : "memory");
      SB();
      if (st == 0) {
#pragma unroll
        for (int u = PD - IORD < TW ? TW : PD - IORD; u < PD; ++u) issue(u);
      }
      if (st + PD < 12) issue(st + PD);
      if (LHI && st + 1 == LHI) { __builtin_amdgcn_s_setprio(PRIO); SB(); }
    }
#pragma unroll
    for (int c = 0; c < 2 * HO; ++c) {
#pragma unroll 1
      for (int spin = 0; spin < (1 << 20); ++spin) {
        if (__builtin_amdgcn_readfirstlane(*(volatile LAS unsigned*)(shm + L_RDY + 52 + 4 * c)) >> 8 * wv & 1) break;
        if (HSL) __builtin_amdgcn_s_sleep(HSL);
      }
      asm volatile("" ::: "memory");
      const rsrc_t rY = RSRC(ARG(6), cb0 + c >= yg * 3 ? 2048 * F_ * 2 : 0);
#pragma unroll
      for (int i = 0; i < 2; ++i) {
        const u32x4 yv = *(LAS u32x4*)(shm + Y_SCR + (c + 1) * 10240 + wv * 2560 + 80 * (lane >> 2) + 16 * (lane & 3) + 1280 * i);
        ST(yv, rY, (lane >> 2) * (F_ * 2) + (lane & 3) * 16 + 16 * i * (F_ * 2), (stile * 128 + wv * 32) * (F_ * 2) + (cb0 + c) * 64, AUXY);
      }
    }
  }
}

extern "C" void kernel_launch(void* const* d_in, const int* in_sizes, int n_in, void* d_out, int out_size,
                              void* d_ws, size_t ws_size, hipStream_t stream) {
  (void)hipFuncSetAttribute((const void*)fused, hipFuncAttributeMaxDynamicSharedMemorySize, LDS_BYTES);
  fused<<<256, 512, LDS_BYTES, stream>>>((const char*)d_in[0], (const char*)d_in[1], (const char*)d_in[2], (const char*)d_in[3],
                                         (const char*)d_in[4], (const char*)d_in[5], (char*)d_out, (char*)d_ws);
}
```
